```python
import torch

def _softmax(x, axis=-1):
    return torch.softmax(x, dim=axis)
def _gelu(x):
    return torch.nn.functional.gelu(x, approximate="tanh")
def _silu(x):
    return torch.nn.functional.silu(x)
def _sigmoid(x):
    return torch.sigmoid(x)
def _layernorm(x, axis=-1, eps=1e-5):
    m = x.mean(dim=axis, keepdim=True)
    v = x.var(dim=axis, keepdim=True, unbiased=False)
    return (x - m) / torch.sqrt(v + eps)
def _scatter_add(tgt, idx, src, axis=0):
    return tgt.clone().index_add_(axis, idx, src)
def _segment_sum(src, seg, n):
    out = torch.zeros((n,) + src.shape[1:], dtype=src.dtype)
    return out.index_add_(0, seg, src)
def _pad(x, pw):
    flat = tuple(v for pair in reversed(pw) for v in pair)
    return torch.nn.functional.pad(x, flat)

kernel_name = 'standalone__sliding_block_cached__decode'

def setup_inputs():

    _w = (lambda a: (lambda t: t.to(torch.bfloat16) if t.is_floating_point() else t)(torch.as_tensor(a)))
    B, Sq, D = 1, 1, 512
    Hq, Hkv, Sk, Dh = 32, 8, 32768, 128
    assert Hq % Hkv == 0, (Hq, Hkv)
    return {
        "x": _w((lambda *s: torch.randn(*s, dtype=torch.bfloat16) * 0.8)(B, Sq, D)),
        "k_cache": _w((lambda *s: torch.randn(*s, dtype=torch.bfloat16) * 0.8)(B, Hkv, Sk, Dh)),
        "v_cache": _w((lambda *s: torch.randn(*s, dtype=torch.bfloat16) * 0.05)(B, Hkv, Sk, Dh)),
        "wq": _w((lambda *s: torch.randn(*s, dtype=torch.bfloat16) * 0.8)(D, Hq * Dh) * (D ** -0.5)),
        "wo": _w((lambda *s: torch.randn(*s, dtype=torch.bfloat16) * 0.05)(Hq * Dh, D) * ((Hq * Dh) ** -0.5)),
    }

def reference(x, k_cache, v_cache, wq, wo):
    B, Sq, D = x.shape
    Hq, Dh = 32, 128
    Hkv, Sk = k_cache.shape[1], k_cache.shape[2]
    W = min(4096, max(256, Sk // 4))
    rep = Hq // Hkv
    q = (lambda x, *d: x.permute(*d))((x @ wq).reshape(B, Sq, Hq, Dh), 0, 2, 1, 3)
    k = (lambda x, n, axis: torch.repeat_interleave(x, n, dim=axis))(k_cache[:, :, Sk - W:, :], rep, axis=1)
    v = (lambda x, n, axis: torch.repeat_interleave(x, n, dim=axis))(v_cache[:, :, Sk - W:, :], rep, axis=1)
    scores = torch.einsum("bhqd,bhkd->bhqk", q, k) * (Dh ** -0.5)
    p = _softmax(scores, axis=-1)
    o = torch.einsum("bhqk,bhkd->bhqd", p, v)
    return (lambda x, *d: x.permute(*d))(o, 0, 2, 1, 3).reshape(B, Sq, Hq * Dh) @ wo
```

```cpp
#include <hip/hip_runtime.h>
#include <hip/hip_bf16.h>
#include <cstdint>

typedef unsigned u32x4 __attribute__((ext_vector_type(4)));
using bf16x2 = __attribute__((ext_vector_type(2))) __bf16;

constexpr int D_MODEL = 512, HQ = 32, HKV = 8, SK = 32768, DH = 128, W = 4096;
constexpr int NSPLIT = 32, KPS = W / NSPLIT;

__device__ __forceinline__ float bf2f(unsigned short b) { return __uint_as_float(((unsigned)b) << 16); }
__device__ __forceinline__ float rbf(float x) { return __bfloat162float(__float2bfloat16(x)); }

__global__ void __launch_bounds__(256) k_qproj(const unsigned short* x, const unsigned short* wq, float* q) {
  __shared__ float red[256 * 8];
  const int t = threadIdx.x, b = blockIdx.x;
  const int half = t & 1, rg = t >> 1;
  float acc[8] = {0};
  for (int j = 0; j < 4; ++j) {
    int r = rg + 128 * j;
    u32x4 w = *(const u32x4*)(wq + (size_t)r * 4096 + b * 16 + half * 8);
    float xv = bf2f(x[r]);
    for (int k = 0; k < 4; ++k) {
      acc[2 * k] += xv * __uint_as_float(w[k] << 16);
      acc[2 * k + 1] += xv * __uint_as_float(w[k] & 0xffff0000u);
    }
  }
  for (int k = 0; k < 8; ++k) red[t * 8 + k] = acc[k];
  __syncthreads();
  if (t < 16) {
    float s = 0;
    int h = t >> 3, k = t & 7;
    for (int i = 0; i < 128; ++i) s += red[(2 * i + h) * 8 + k];
    q[b * 16 + t] = rbf(s);
  }
}

__global__ void __launch_bounds__(256) k_attn(const float* q, const unsigned short* kc, const unsigned short* vc, float* po, float* pml) {
  __shared__ float qs[4 * 128];
  __shared__ float sc[4 * 128];
  __shared__ float mx[4], sm[4];
  const int t = threadIdx.x, g = blockIdx.x & 7, s = blockIdx.x >> 3;
  for (int i = t; i < 512; i += 256) qs[i] = q[g * 512 + i];
  __syncthreads();
  const size_t base = ((size_t)g * SK + (SK - W) + (size_t)s * KPS) * DH;
  {
    int key = t >> 1, hf = t & 1;
    float a[4] = {0, 0, 0, 0};
    for (int j = 0; j < 8; ++j) {
      u32x4 w = *(const u32x4*)(kc + base + (size_t)key * DH + hf * 64 + j * 8);
      for (int k = 0; k < 4; ++k) {
        float lo = __uint_as_float(w[k] << 16), hi = __uint_as_float(w[k] & 0xffff0000u);
        int d = hf * 64 + j * 8 + 2 * k;
        for (int h = 0; h < 4; ++h) a[h] += lo * qs[h * 128 + d] + hi * qs[h * 128 + d + 1];
      }
    }
    for (int h = 0; h < 4; ++h) {
      a[h] += __shfl_xor(a[h], 1);
      if (hf == 0) sc[h * 128 + key] = rbf(rbf(a[h]) * 0.08838834764831845f);
    }
  }
  __syncthreads();
  if (t < 4) {
    float m = -INFINITY;
    for (int i = 0; i < 128; ++i) m = fmaxf(m, sc[t * 128 + i]);
    float l = 0;
    for (int i = 0; i < 128; ++i) { float p = __expf(sc[t * 128 + i] - m); sc[t * 128 + i] = p; l += p; }
    mx[t] = m; sm[t] = l;
  }
  __syncthreads();
  {
    int d = t & 127, hp = t >> 7;
    float a0 = 0, a1 = 0;
    for (int k = 0; k < 128; ++k) {
      float v = bf2f(vc[base + (size_t)k * DH + d]);
      a0 += sc[(2 * hp) * 128 + k] * v;
      a1 += sc[(2 * hp + 1) * 128 + k] * v;
    }
    int h0 = g * 4 + 2 * hp;
    po[((size_t)h0 * NSPLIT + s) * 128 + d] = a0;
    po[((size_t)(h0 + 1) * NSPLIT + s) * 128 + d] = a1;
    if (t < 4) { pml[((g * 4 + t) * NSPLIT + s) * 2] = mx[t]; pml[((g * 4 + t) * NSPLIT + s) * 2 + 1] = sm[t]; }
  }
}

__global__ void __launch_bounds__(256) k_oproj(const float* po, const float* pml, const unsigned short* wo, float* pout) {
  __shared__ float os[16];
  const int t = threadIdx.x, b = blockIdx.x;
  const int h = b >> 3, j0 = (b & 7) * 16;
  if (t < 16) {
    float M = -INFINITY;
    for (int s = 0; s < NSPLIT; ++s) M = fmaxf(M, pml[(h * NSPLIT + s) * 2]);
    float L = 0, o = 0;
    for (int s = 0; s < NSPLIT; ++s) {
      float w = __expf(pml[(h * NSPLIT + s) * 2] - M);
      L += w * pml[(h * NSPLIT + s) * 2 + 1];
      o += w * po[((size_t)h * NSPLIT + s) * 128 + j0 + t];
    }
    os[t] = rbf(o / L);
  }
  __syncthreads();
  float a0 = 0, a1 = 0;
  for (int r = 0; r < 16; ++r) {
    unsigned w = *(const unsigned*)(wo + (size_t)(b * 16 + r) * 512 + 2 * t);
    a0 += os[r] * __uint_as_float(w << 16);
    a1 += os[r] * __uint_as_float(w & 0xffff0000u);
  }
  pout[b * 512 + 2 * t] = a0;
  pout[b * 512 + 2 * t + 1] = a1;
}

__global__ void __launch_bounds__(256) k_final(const float* pout, __hip_bfloat16* out) {
  __shared__ float red[256];
  const int t = threadIdx.x, b = blockIdx.x;
  int col = b * 64 + (t & 63), part = t >> 6;
  float a = 0;
  for (int p = part; p < 256; p += 4) a += pout[p * 512 + col];
  red[t] = a;
  __syncthreads();
  if (t < 64) out[col] = __float2bfloat16(red[t] + red[t + 64] + red[t + 128] + red[t + 192]);
}

extern "C" void kernel_launch(void* const* d_in, const int* in_sizes, int n_in, void* d_out, int out_size, void* d_ws, size_t ws_size, hipStream_t stream) {
  const unsigned short* x = (const unsigned short*)d_in[0];
  const unsigned short* kc = (const unsigned short*)d_in[1];
  const unsigned short* vc = (const unsigned short*)d_in[2];
  const unsigned short* wq = (const unsigned short*)d_in[3];
  const unsigned short* wo = (const unsigned short*)d_in[4];
  float* ws = (float*)d_ws;
  float* q = ws;
  float* pml = ws + 4096;
  float* po = ws + 8192;
  float* pout = ws + 8192 + 131072;
  k_qproj<<<256, 256, 0, stream>>>(x, wq, q);
  k_attn<<<256, 256, 0, stream>>>(q, kc, vc, po, pml);
  k_oproj<<<256, 256, 0, stream>>>(po, pml, wo, pout);
  k_final<<<8, 256, 0, stream>>>(pout, (__hip_bfloat16*)d_out);
}
```

```cpp
#include <hip/hip_runtime.h>
#include <hip/hip_bf16.h>
#include <cstdint>

typedef unsigned u32x4 __attribute__((ext_vector_type(4)));
typedef unsigned u32x2 __attribute__((ext_vector_type(2)));
typedef float f32x4 __attribute__((ext_vector_type(4)));
typedef float f32x2 __attribute__((ext_vector_type(2)));
using bf16x2 = __attribute__((ext_vector_type(2))) __bf16;
typedef unsigned short u16;

constexpr int D_MODEL = 512, HQ = 32, HKV = 8, SK = 32768, DH = 128, W = 4096;
constexpr int NSPLIT = 32, KPS = W / NSPLIT;
constexpr int NW = 8, NT = (NW + 1) * 64;
constexpr unsigned SPIN_CAP = 1u << 22;

constexpr unsigned EPOCH_OFF = 0;
constexpr unsigned Q_OFF = 4096;
constexpr unsigned ML_OFF = Q_OFF + 16384;
constexpr unsigned P_OFF = ML_OFF + 16384;
constexpr unsigned O_OFF = P_OFF + (1u << 20);
constexpr unsigned WS_BYTES = O_OFF + (1u << 20);
constexpr int AUX_SC1 = 16;

__device__ __forceinline__ float bflo(unsigned w) { return __uint_as_float(w << 16); }
__device__ __forceinline__ float bfhi(unsigned w) { return __uint_as_float(w & 0xffff0000u); }
__device__ __forceinline__ float rbf(float x) { return __bfloat162float(__float2bfloat16(x)); }
__device__ __forceinline__ unsigned packbf(float lo, float hi) {
  return (unsigned)__bfloat16_as_ushort(__float2bfloat16(lo)) | ((unsigned)__bfloat16_as_ushort(__float2bfloat16(hi)) << 16);
}
__device__ __forceinline__ float dot2(unsigned k, unsigned q, float acc) {
  return __builtin_amdgcn_fdot2_f32_bf16(__builtin_bit_cast(bf16x2, k), __builtin_bit_cast(bf16x2, q), acc, false);
}
template <int CTRL> __device__ __forceinline__ float dpp(float x) {
  return __builtin_bit_cast(float, __builtin_amdgcn_mov_dpp(__builtin_bit_cast(int, x), CTRL, 0xf, 0xf, true));
}
constexpr int XOR1 = 0xB1, XOR2 = 0x4E, XOR7 = 0x141, XOR8 = 0x128, ROR4 = 0x124;
__device__ __forceinline__ float sw16_add(float a, float b) {
  auto s = __builtin_amdgcn_permlane16_swap(__float_as_uint(a), __float_as_uint(b), false, false);
  return __uint_as_float(s[0]) + __uint_as_float(s[1]);
}
__device__ __forceinline__ float sw32_add(float a, float b) {
  auto s = __builtin_amdgcn_permlane32_swap(__float_as_uint(a), __float_as_uint(b), false, false);
  return __uint_as_float(s[0]) + __uint_as_float(s[1]);
}
__device__ __forceinline__ float sw16_max(float a) {
  auto s = __builtin_amdgcn_permlane16_swap(__float_as_uint(a), __float_as_uint(a), false, false);
  return fmaxf(__uint_as_float(s[0]), __uint_as_float(s[1]));
}
__device__ __forceinline__ float sw32_max(float a) {
  auto s = __builtin_amdgcn_permlane32_swap(__float_as_uint(a), __float_as_uint(a), false, false);
  return fmaxf(__uint_as_float(s[0]), __uint_as_float(s[1]));
}
__device__ __forceinline__ u32x4 ldnt(const u16* p) { return __builtin_nontemporal_load(reinterpret_cast<const u32x4*>(p)); }

__global__ void __launch_bounds__(NT) fused_decode(const u16* __restrict__ x, const u16* __restrict__ kc, const u16* __restrict__ vc,
                                                   const u16* __restrict__ wq, const u16* __restrict__ wo, unsigned* __restrict__ out,
                                                   unsigned char* ws) {
  __shared__ __attribute__((aligned(16))) float lds_qpart[NW * 16];
  __shared__ __attribute__((aligned(16))) unsigned lds_q[256];
  __shared__ __attribute__((aligned(16))) float lds_p[NW * 64];
  __shared__ __attribute__((aligned(16))) float lds_mrg[NW * 4 * 128];
  __shared__ __attribute__((aligned(16))) float lds_ml[NW * 4 * 2];
  __shared__ __attribute__((aligned(16))) float lds_o16[16];
  __shared__ unsigned lds_tag;

  const int b = blockIdx.x, g = b & 7, s = b >> 3;
  const int tid = threadIdx.x, lane = tid & 63, wave = __builtin_amdgcn_readfirstlane(tid >> 6);
  const __amdgpu_buffer_rsrc_t rs = __builtin_amdgcn_make_buffer_rsrc((void*)ws, 0, (int)WS_BYTES, 0x00020000);
  const int idx = g * 512 + s * 16;
  constexpr float LOG2E = 1.4426950408889634f;
  constexpr float SCALE = 0.08838834764831845f;

  if (wave < NW) {
    const int r = lane >> 4, c = lane & 15;
    const int qr0 = wave * 64 + (lane >> 1), hf = lane & 1;
    const u16 x0 = x[qr0], x1 = x[qr0 + 32];
    const u32x4 wq0 = ldnt(wq + (size_t)qr0 * 4096 + idx + hf * 8);
    const u32x4 wq1 = ldnt(wq + (size_t)(qr0 + 32) * 4096 + idx + hf * 8);
    const size_t kvbase = ((size_t)g * SK + (SK - W) + (size_t)s * KPS + wave * 16 + r) * DH + 8 * c;
    u32x4 K[4], V[4];
#pragma unroll
    for (int i = 0; i < 4; ++i) K[i] = ldnt(kc + kvbase + (size_t)i * 4 * DH);
#pragma unroll
    for (int i = 0; i < 4; ++i) V[i] = ldnt(vc + kvbase + (size_t)i * 4 * DH);
    const int r8 = lane >> 3, c8 = lane & 7;
    const u32x4 wo0 = ldnt(wo + (size_t)(idx + r8) * 512 + wave * 64 + c8 * 8);
    const u32x4 wo1 = ldnt(wo + (size_t)(idx + r8 + 8) * 512 + wave * 64 + c8 * 8);
    {
      const float xf0 = bflo(x0), xf1 = bflo(x1);
      float a[8];
#pragma unroll
      for (int k = 0; k < 4; ++k) {
        a[2 * k] = xf0 * bflo(wq0[k]) + xf1 * bflo(wq1[k]);
        a[2 * k + 1] = xf0 * bfhi(wq0[k]) + xf1 * bfhi(wq1[k]);
      }
#pragma unroll
      for (int k = 0; k < 8; ++k) {
        float t = a[k];
        t += dpp<XOR2>(t); t += dpp<ROR4>(t); t += dpp<XOR8>(t);
        t = sw16_add(t, t); t = sw32_add(t, t);
        a[k] = t;
      }
      if (lane < 2) {
        *reinterpret_cast<f32x4*>(&lds_qpart[wave * 16 + lane * 8]) = f32x4{a[0], a[1], a[2], a[3]};
        *reinterpret_cast<f32x4*>(&lds_qpart[wave * 16 + lane * 8 + 4]) = f32x4{a[4], a[5], a[6], a[7]};
      }
    }
    __syncthreads();
    __syncthreads();
    const unsigned tag = lds_tag;
    u32x4 q[4];
#pragma unroll
    for (int h = 0; h < 4; ++h) q[h] = *reinterpret_cast<const u32x4*>(&lds_q[h * 64 + c * 4]);
    float xs[16];
#pragma unroll
    for (int i = 0; i < 4; ++i)
#pragma unroll
      for (int h = 0; h < 4; ++h) {
        float d = 0.f;
#pragma unroll
        for (int k = 0; k < 4; ++k) d = dot2(K[i][k], q[h][k], d);
        xs[i * 4 + h] = d;
      }
#define TR_STEP(HALF, CTRL, BIT) _Pragma("unroll") for (int h = 0; h < (HALF); ++h) { \
      const float lo_ = xs[h], hi_ = xs[h + (HALF)]; const float t1_ = lo_ + dpp<CTRL>(lo_), t2_ = hi_ + dpp<CTRL>(hi_); \
      xs[h] = (c & (BIT)) ? t2_ : t1_; }
    TR_STEP(8, XOR8, 8) TR_STEP(4, XOR7, 4) TR_STEP(2, XOR2, 2) TR_STEP(1, XOR1, 1)
#undef TR_STEP
    const float sc = rbf(rbf(xs[0]) * SCALE) * LOG2E;
    float m = sc; m = fmaxf(m, dpp<ROR4>(m)); m = fmaxf(m, dpp<XOR8>(m)); m = sw16_max(m); m = sw32_max(m);
    const float p = __builtin_amdgcn_exp2f(sc - m);
    float l = p; l += dpp<ROR4>(l); l += dpp<XOR8>(l); l = sw16_add(l, l); l = sw32_add(l, l);
    float* prow = &lds_p[(wave * 4 + r) * 16];
    prow[c] = p;
    asm volatile("s_waitcnt lgkmcnt(0)" ::: "memory");
    f32x4 pr[4];
#pragma unroll
    for (int i = 0; i < 4; ++i) pr[i] = *reinterpret_cast<const f32x4*>(prow + 4 * i);
    float acc[32];
#pragma unroll
    for (int k = 0; k < 32; ++k) acc[k] = 0.f;
#pragma unroll
    for (int i = 0; i < 4; ++i) {
      float vf[8];
#pragma unroll
      for (int k = 0; k < 4; ++k) { vf[2 * k] = bflo(V[i][k]); vf[2 * k + 1] = bfhi(V[i][k]); }
#pragma unroll
      for (int h = 0; h < 4; ++h)
#pragma unroll
        for (int k = 0; k < 8; ++k) acc[h * 8 + k] = fmaf(pr[i][h], vf[k], acc[h * 8 + k]);
    }
    float t2[16], o8[8];
#pragma unroll
    for (int h = 0; h < 2; ++h)
#pragma unroll
      for (int k = 0; k < 8; ++k) t2[h * 8 + k] = sw32_add(acc[h * 8 + k], acc[(h + 2) * 8 + k]);
#pragma unroll
    for (int k = 0; k < 8; ++k) o8[k] = sw16_add(t2[k], t2[8 + k]);
    *reinterpret_cast<f32x4*>(&lds_mrg[(wave * 4 + r) * 128 + 8 * c]) = f32x4{o8[0], o8[1], o8[2], o8[3]};
    *reinterpret_cast<f32x4*>(&lds_mrg[(wave * 4 + r) * 128 + 8 * c + 4]) = f32x4{o8[4], o8[5], o8[6], o8[7]};
    if (lane < 4) { lds_ml[(wave * 4 + lane) * 2] = m; lds_ml[(wave * 4 + lane) * 2 + 1] = l; }
    __syncthreads();
    if (wave < 4) {
      const int h = wave, d0 = 2 * lane;
      float M = -INFINITY;
#pragma unroll
      for (int w = 0; w < NW; ++w) M = fmaxf(M, lds_ml[(w * 4 + h) * 2]);
      float L = 0.f, o0 = 0.f, o1 = 0.f;
#pragma unroll
      for (int w = 0; w < NW; ++w) {
        const float wt = __builtin_amdgcn_exp2f(lds_ml[(w * 4 + h) * 2] - M);
        const f32x2 v = *reinterpret_cast<const f32x2*>(&lds_mrg[(w * 4 + h) * 128 + d0]);
        L = fmaf(wt, lds_ml[(w * 4 + h) * 2 + 1], L); o0 = fmaf(wt, v[0], o0); o1 = fmaf(wt, v[1], o1);
      }
      const int hg = g * 4 + h;
      const unsigned off = P_OFF + (unsigned)((((hg * 8 + (d0 >> 4)) * 32 + s) * 16 + (d0 & 15)) * 8);
      __builtin_amdgcn_raw_buffer_store_b128(u32x4{__float_as_uint(o0), tag, __float_as_uint(o1), tag}, rs, off, 0, AUX_SC1);
      if (lane == 0)
        __builtin_amdgcn_raw_buffer_store_b128(u32x4{__float_as_uint(M), tag, __float_as_uint(L), tag}, rs, ML_OFF + (unsigned)((hg * 32 + s) * 16), 0, AUX_SC1);
    }
    __syncthreads();
    {
      const float oa = lds_o16[r8], ob = lds_o16[r8 + 8];
      float a[8];
#pragma unroll
      for (int k = 0; k < 4; ++k) {
        a[2 * k] = oa * bflo(wo0[k]) + ob * bflo(wo1[k]);
        a[2 * k + 1] = oa * bfhi(wo0[k]) + ob * bfhi(wo1[k]);
      }
      float t4[4], u2[2];
#pragma unroll
      for (int k = 0; k < 4; ++k) t4[k] = sw32_add(a[k], a[k + 4]);
#pragma unroll
      for (int k = 0; k < 2; ++k) { u2[k] = sw16_add(t4[k], t4[k + 2]); u2[k] += dpp<XOR8>(u2[k]); }
      const int rho = lane >> 4;
      const int col = wave * 64 + 8 * c8 + 4 * (rho >> 1) + 2 * (rho & 1);
      if ((lane & 8) == 0)
        __builtin_amdgcn_raw_buffer_store_b128(u32x4{__float_as_uint(u2[0]), tag, __float_as_uint(u2[1]), tag}, rs, O_OFF + (unsigned)((b * 512 + col) * 8), 0, AUX_SC1);
    }
  } else {
    const unsigned epoch = __hip_atomic_load(reinterpret_cast<const unsigned*>(ws + EPOCH_OFF), __ATOMIC_RELAXED, __HIP_MEMORY_SCOPE_AGENT);
    const unsigned tag = epoch + 1u;
    if (lane == 0) lds_tag = tag;
    __syncthreads();
    if (lane < 4) {
      f32x4 a = *reinterpret_cast<const f32x4*>(&lds_qpart[lane * 4]);
#pragma unroll
      for (int w = 1; w < NW; ++w) a += *reinterpret_cast<const f32x4*>(&lds_qpart[w * 16 + lane * 4]);
      __builtin_amdgcn_raw_buffer_store_b128(u32x4{packbf(a[0], a[1]), tag, packbf(a[2], a[3]), tag}, rs, Q_OFF + (unsigned)(g * 2048 + s * 64 + lane * 16), 0, AUX_SC1);
    }
    {
      u32x4 v0, v1;
      for (unsigned spins = 0; spins < SPIN_CAP; ++spins) {
        asm volatile("" ::: "memory");
        v0 = __builtin_amdgcn_raw_buffer_load_b128(rs, Q_OFF + (unsigned)(g * 2048 + lane * 16), 0, AUX_SC1);
        v1 = __builtin_amdgcn_raw_buffer_load_b128(rs, Q_OFF + (unsigned)(g * 2048 + 1024 + lane * 16), 0, AUX_SC1);
        const bool ok = v0[1] == tag && v0[3] == tag && v1[1] == tag && v1[3] == tag;
        if (__all(ok)) break;
      }
      *reinterpret_cast<u32x2*>(&lds_q[lane * 2]) = u32x2{v0[0], v0[2]};
      *reinterpret_cast<u32x2*>(&lds_q[128 + lane * 2]) = u32x2{v1[0], v1[2]};
    }
    __syncthreads();
    __syncthreads();
    {
      const int hg = g * 4 + (s >> 3), j = s & 7;
      u32x4 pv[4], ml[4];
      for (unsigned spins = 0; spins < SPIN_CAP; ++spins) {
        asm volatile("" ::: "memory");
        bool ok = true;
#pragma unroll
        for (int i = 0; i < 4; ++i) {
          pv[i] = __builtin_amdgcn_raw_buffer_load_b128(rs, P_OFF + (unsigned)((hg * 8 + j) * 4096 + (lane + 64 * i) * 16), 0, AUX_SC1);
          ml[i] = __builtin_amdgcn_raw_buffer_load_b128(rs, ML_OFF + (unsigned)((hg * 32 + (lane >> 3) + 8 * i) * 16), 0, AUX_SC1);
        }
#pragma unroll
        for (int i = 0; i < 4; ++i) ok = ok && pv[i][1] == tag && pv[i][3] == tag && ml[i][1] == tag && ml[i][3] == tag;
        if (__all(ok)) break;
      }
      float M = fmaxf(fmaxf(__uint_as_float(ml[0][0]), __uint_as_float(ml[1][0])), fmaxf(__uint_as_float(ml[2][0]), __uint_as_float(ml[3][0])));
      M = fmaxf(M, dpp<XOR8>(M)); M = sw16_max(M); M = sw32_max(M);
      float L = 0.f, o0 = 0.f, o1 = 0.f;
#pragma unroll
      for (int i = 0; i < 4; ++i) {
        const float wt = __builtin_amdgcn_exp2f(__uint_as_float(ml[i][0]) - M);
        L = fmaf(wt, __uint_as_float(ml[i][2]), L);
        o0 = fmaf(wt, __uint_as_float(pv[i][0]), o0);
        o1 = fmaf(wt, __uint_as_float(pv[i][2]), o1);
      }
      L += dpp<XOR8>(L); L = sw16_add(L, L); L = sw32_add(L, L);
      o0 += dpp<XOR8>(o0); o0 = sw16_add(o0, o0); o0 = sw32_add(o0, o0);
      o1 += dpp<XOR8>(o1); o1 = sw16_add(o1, o1); o1 = sw32_add(o1, o1);
      if (lane < 8) *reinterpret_cast<f32x2*>(&lds_o16[lane * 2]) = f32x2{rbf(o0 / L), rbf(o1 / L)};
    }
    __syncthreads();
    {
      u32x4 ov[4];
      for (unsigned spins = 0; spins < SPIN_CAP; ++spins) {
        asm volatile("" ::: "memory");
        bool ok = true;
#pragma unroll
        for (int i = 0; i < 4; ++i) ov[i] = __builtin_amdgcn_raw_buffer_load_b128(rs, O_OFF + (unsigned)(((lane + 64 * i) * 512 + 2 * b) * 8), 0, AUX_SC1);
#pragma unroll
        for (int i = 0; i < 4; ++i) ok = ok && ov[i][1] == tag && ov[i][3] == tag;
        if (__all(ok)) break;
      }
      float a0 = (__uint_as_float(ov[0][0]) + __uint_as_float(ov[1][0])) + (__uint_as_float(ov[2][0]) + __uint_as_float(ov[3][0]));
      float a1 = (__uint_as_float(ov[0][2]) + __uint_as_float(ov[1][2])) + (__uint_as_float(ov[2][2]) + __uint_as_float(ov[3][2]));
      a0 += dpp<XOR1>(a0); a0 += dpp<XOR2>(a0); a0 += dpp<ROR4>(a0); a0 += dpp<XOR8>(a0); a0 = sw16_add(a0, a0); a0 = sw32_add(a0, a0);
      a1 += dpp<XOR1>(a1); a1 += dpp<XOR2>(a1); a1 += dpp<ROR4>(a1); a1 += dpp<XOR8>(a1); a1 = sw16_add(a1, a1); a1 = sw32_add(a1, a1);
      if (lane == 0) {
        out[b] = packbf(a0, a1);
        if (b == 0) __hip_atomic_store(reinterpret_cast<unsigned*>(ws + EPOCH_OFF), tag, __ATOMIC_RELAXED, __HIP_MEMORY_SCOPE_AGENT);
      }
    }
  }
}

extern "C" void kernel_launch(void* const* d_in, const int* in_sizes, int n_in, void* d_out, int out_size, void* d_ws, size_t ws_size, hipStream_t stream) {
  fused_decode<<<256, NT, 0, stream>>>((const u16*)d_in[0], (const u16*)d_in[1], (const u16*)d_in[2], (const u16*)d_in[3], (const u16*)d_in[4],
                                       (unsigned*)d_out, (unsigned char*)d_ws);
}
```

```cpp
#include <hip/hip_runtime.h>
#include <hip/hip_bf16.h>
#include <cstdint>

typedef unsigned u32x4 __attribute__((ext_vector_type(4)));
typedef unsigned u32x2 __attribute__((ext_vector_type(2)));
typedef float f32x4 __attribute__((ext_vector_type(4)));
typedef float f32x2 __attribute__((ext_vector_type(2)));
typedef __bf16 bf16x8 __attribute__((ext_vector_type(8)));
typedef short v4i16_t __attribute__((ext_vector_type(4)));
typedef unsigned short u16;
#define LAS __attribute__((address_space(3)))

constexpr int D_MODEL = 512, HQ = 32, HKV = 8, SK = 32768, DH = 128, W = 4096;
constexpr int NSPLIT = 32, KPS = W / NSPLIT;
constexpr int NW = 8, NT = (NW + 1) * 64;
constexpr unsigned SPIN_CAP = 1u << 22;

constexpr unsigned EPOCH_OFF = 0;
constexpr unsigned Q_OFF = 4096;
constexpr unsigned ML_OFF = Q_OFF + 16384;
constexpr unsigned P_OFF = ML_OFF + 16384;
constexpr unsigned O_OFF = P_OFF + (1u << 20);
constexpr unsigned WS_BYTES = O_OFF + (1u << 20);
constexpr int AUX_SC1 = 16;
#define STAMP(k)
#define STAMP_DECL
#define STAMP_OUT(lo, hi)

__device__ __forceinline__ float bflo(unsigned w) { return __uint_as_float(w << 16); }
__device__ __forceinline__ float bfhi(unsigned w) { return __uint_as_float(w & 0xffff0000u); }
__device__ __forceinline__ float rbf(float x) { return __bfloat162float(__float2bfloat16(x)); }
__device__ __forceinline__ unsigned packbf(float lo, float hi) {
  return (unsigned)__bfloat16_as_ushort(__float2bfloat16(lo)) | ((unsigned)__bfloat16_as_ushort(__float2bfloat16(hi)) << 16);
}
template <int CTRL> __device__ __forceinline__ float dpp(float x) {
  return __builtin_bit_cast(float, __builtin_amdgcn_mov_dpp(__builtin_bit_cast(int, x), CTRL, 0xf, 0xf, true));
}
constexpr int XOR1 = 0xB1, XOR2 = 0x4E, XOR7 = 0x141, XOR8 = 0x128, ROR4 = 0x124;
__device__ __forceinline__ float sw16_add(float a, float b) {
  auto s = __builtin_amdgcn_permlane16_swap(__float_as_uint(a), __float_as_uint(b), false, false);
  return __uint_as_float(s[0]) + __uint_as_float(s[1]);
}
__device__ __forceinline__ float sw32_add(float a, float b) {
  auto s = __builtin_amdgcn_permlane32_swap(__float_as_uint(a), __float_as_uint(b), false, false);
  return __uint_as_float(s[0]) + __uint_as_float(s[1]);
}
__device__ __forceinline__ float sw16_max(float a) {
  auto s = __builtin_amdgcn_permlane16_swap(__float_as_uint(a), __float_as_uint(a), false, false);
  return fmaxf(__uint_as_float(s[0]), __uint_as_float(s[1]));
}
__device__ __forceinline__ float sw32_max(float a) {
  auto s = __builtin_amdgcn_permlane32_swap(__float_as_uint(a), __float_as_uint(a), false, false);
  return fmaxf(__uint_as_float(s[0]), __uint_as_float(s[1]));
}
__device__ __forceinline__ u32x4 ldnt(const u16* p) { return __builtin_nontemporal_load(reinterpret_cast<const u32x4*>(p)); }
__device__ __forceinline__ unsigned off_b(unsigned row, unsigned ch) { return 256u * row + 16u * (ch ^ (((row & 3) << 2) | ((row >> 2) & 3))); }
__device__ __forceinline__ float sel4(f32x4 d, int g) { return g == 0 ? d[0] : g == 1 ? d[1] : g == 2 ? d[2] : d[3]; }
#define CBAR() asm volatile("" ::: "memory")

__global__ void __launch_bounds__(NT) fused_decode(const u16* __restrict__ x, const u16* __restrict__ kc, const u16* __restrict__ vc,
                                                   const u16* __restrict__ wq, const u16* __restrict__ wo, unsigned* __restrict__ out,
                                                   unsigned char* ws) {
  __shared__ __attribute__((aligned(16))) unsigned char lds_v[128 * 256];
  __shared__ __attribute__((aligned(16))) float lds_qpart[NW * 16];
  __shared__ __attribute__((aligned(16))) unsigned lds_q[256];
  __shared__ __attribute__((aligned(16))) u16 lds_pm[4 * 128];
  __shared__ __attribute__((aligned(16))) float lds_m[4 * 8];
  __shared__ __attribute__((aligned(16))) float lds_l[4 * 8];
  __shared__ __attribute__((aligned(16))) float lds_o16[16];
  __shared__ unsigned lds_tag;

  const int b = blockIdx.x, g = b & 7, s = b >> 3;
  const int tid = threadIdx.x, lane = tid & 63, wave = __builtin_amdgcn_readfirstlane(tid >> 6);
  const __amdgpu_buffer_rsrc_t rs = __builtin_amdgcn_make_buffer_rsrc((void*)ws, 0, (int)WS_BYTES, 0x00020000);
  const int idx = g * 512 + s * 16;
  constexpr float LOG2E = 1.4426950408889634f;
  constexpr float SCALE = 0.08838834764831845f;
  STAMP_DECL;

  if (wave < NW) {
    STAMP(10);
    const int G = lane >> 4, n = lane & 15;
    const int qr0 = wave * 64 + (lane >> 1), hf = lane & 1;
    const u32x4 wq0 = ldnt(wq + (size_t)qr0 * 4096 + idx + hf * 8);
    const u32x4 wq1 = ldnt(wq + (size_t)(qr0 + 32) * 4096 + idx + hf * 8);
    const u16 x0 = x[qr0], x1 = x[qr0 + 32];
    CBAR(); __builtin_amdgcn_s_barrier(); CBAR();
    const size_t kvrow = (size_t)g * SK + (SK - W) + (size_t)s * KPS + wave * 16;
    u32x4 K[4], V[4];
#pragma unroll
    for (int t = 0; t < 4; ++t) K[t] = ldnt(kc + (kvrow + n) * DH + 32 * t + 8 * G);
    CBAR(); __builtin_amdgcn_s_barrier(); CBAR();
#pragma unroll
    for (int i = 0; i < 4; ++i) V[i] = ldnt(vc + (kvrow + 4 * i + G) * DH + 8 * n);
    CBAR(); __builtin_amdgcn_s_barrier(); CBAR();
    const int r8 = lane >> 3, c8 = lane & 7;
    const u32x4 wo0 = ldnt(wo + (size_t)(idx + r8) * 512 + wave * 64 + c8 * 8);
    const u32x4 wo1 = ldnt(wo + (size_t)(idx + r8 + 8) * 512 + wave * 64 + c8 * 8);
    CBAR();
    {
      const float xf0 = bflo(x0), xf1 = bflo(x1);
      float a[8];
#pragma unroll
      for (int k = 0; k < 4; ++k) {
        a[2 * k] = xf0 * bflo(wq0[k]) + xf1 * bflo(wq1[k]);
        a[2 * k + 1] = xf0 * bfhi(wq0[k]) + xf1 * bfhi(wq1[k]);
      }
#pragma unroll
      for (int k = 0; k < 8; ++k) {
        float t = a[k];
        t += dpp<XOR2>(t); t += dpp<ROR4>(t); t += dpp<XOR8>(t);
        t = sw16_add(t, t); t = sw32_add(t, t);
        a[k] = t;
      }
      if (lane < 2) {
        *reinterpret_cast<f32x4*>(&lds_qpart[wave * 16 + lane * 8]) = f32x4{a[0], a[1], a[2], a[3]};
        *reinterpret_cast<f32x4*>(&lds_qpart[wave * 16 + lane * 8 + 4]) = f32x4{a[4], a[5], a[6], a[7]};
      }
    }
    STAMP(11);
    __syncthreads();
    __syncthreads();
    STAMP(12);
    const unsigned tag = lds_tag;
    f32x4 sacc = {0.f, 0.f, 0.f, 0.f};
#pragma unroll
    for (int t = 0; t < 4; ++t) {
      const u32x4 qa = *reinterpret_cast<const u32x4*>(&lds_q[(lane & 3) * 64 + 16 * t + 4 * G]);
      sacc = __builtin_amdgcn_mfma_f32_16x16x32_bf16(__builtin_bit_cast(bf16x8, qa), __builtin_bit_cast(bf16x8, K[t]), sacc, 0, 0, 0);
    }
    const float sc = rbf(rbf(sel4(sacc, G)) * SCALE) * LOG2E;
    float m = sc; m = fmaxf(m, dpp<XOR1>(m)); m = fmaxf(m, dpp<XOR2>(m)); m = fmaxf(m, dpp<ROR4>(m)); m = fmaxf(m, dpp<XOR8>(m));
    if (n == 0) lds_m[G * 8 + wave] = m;
    STAMP(13);
    __syncthreads();
    {
      const f32x4 m0 = *reinterpret_cast<const f32x4*>(&lds_m[G * 8]), m1 = *reinterpret_cast<const f32x4*>(&lds_m[G * 8 + 4]);
      m = fmaxf(fmaxf(fmaxf(m0[0], m0[1]), fmaxf(m0[2], m0[3])), fmaxf(fmaxf(m1[0], m1[1]), fmaxf(m1[2], m1[3])));
    }
    const float p = __builtin_amdgcn_exp2f(sc - m);
    lds_pm[G * 128 + wave * 16 + n] = __bfloat16_as_ushort(__float2bfloat16(p));
    float l = p; l += dpp<XOR1>(l); l += dpp<XOR2>(l); l += dpp<ROR4>(l); l += dpp<XOR8>(l);
    if (n == 0) lds_l[G * 8 + wave] = l;
#pragma unroll
    for (int i = 0; i < 4; ++i) *reinterpret_cast<u32x4*>(&lds_v[off_b(wave * 16 + 4 * i + G, n)]) = V[i];
    STAMP(14);
    __syncthreads();
    STAMP(15);
    f32x4 oacc = {0.f, 0.f, 0.f, 0.f};
    {
      const unsigned q_ = (lane & 15) >> 2, p_ = lane & 3;
#pragma unroll
      for (int ks = 0; ks < 4; ++ks) {
        const u32x4 pa = *reinterpret_cast<const u32x4*>(&lds_pm[(lane & 3) * 128 + 32 * ks + 8 * G]);
        const v4i16_t b0 = __builtin_amdgcn_ds_read_tr16_b64_v4i16((LAS v4i16_t*)(&lds_v[off_b(32 * ks + 8 * G + q_, 2 * wave + (p_ >> 1)) + 8 * (p_ & 1)]));
        const v4i16_t b1 = __builtin_amdgcn_ds_read_tr16_b64_v4i16((LAS v4i16_t*)(&lds_v[off_b(32 * ks + 8 * G + 4 + q_, 2 * wave + (p_ >> 1)) + 8 * (p_ & 1)]));
        const u32x2 b0u = __builtin_bit_cast(u32x2, b0), b1u = __builtin_bit_cast(u32x2, b1);
        const u32x4 vb = {b0u[0], b0u[1], b1u[0], b1u[1]};
        oacc = __builtin_amdgcn_mfma_f32_16x16x32_bf16(__builtin_bit_cast(bf16x8, pa), __builtin_bit_cast(bf16x8, vb), oacc, 0, 0, 0);
      }
    }
    {
      const float ov = sel4(oacc, G);
      const float on = dpp<XOR1>(ov);
      const int hg = g * 4 + G;
      if ((n & 1) == 0)
        __builtin_amdgcn_raw_buffer_store_b128(u32x4{__float_as_uint(ov), tag, __float_as_uint(on), tag}, rs,
                                               P_OFF + (unsigned)(((hg * 8 + wave) * 32 + s) * 128 + n * 8), 0, AUX_SC1);
      if (wave == 0 && n == 0) {
        const f32x4 l0 = *reinterpret_cast<const f32x4*>(&lds_l[G * 8]), l1 = *reinterpret_cast<const f32x4*>(&lds_l[G * 8 + 4]);
        const float L = ((l0[0] + l0[1]) + (l0[2] + l0[3])) + ((l1[0] + l1[1]) + (l1[2] + l1[3]));
        __builtin_amdgcn_raw_buffer_store_b128(u32x4{__float_as_uint(m), tag, __float_as_uint(L), tag}, rs, ML_OFF + (unsigned)((hg * 32 + s) * 16), 0, AUX_SC1);
      }
    }
    STAMP(16);
    __syncthreads();
    STAMP(17);
    {
      const float oa = lds_o16[r8], ob = lds_o16[r8 + 8];
      float a[8];
#pragma unroll
      for (int k = 0; k < 4; ++k) {
        a[2 * k] = oa * bflo(wo0[k]) + ob * bflo(wo1[k]);
        a[2 * k + 1] = oa * bfhi(wo0[k]) + ob * bfhi(wo1[k]);
      }
      float t4[4], u2[2];
#pragma unroll
      for (int k = 0; k < 4; ++k) t4[k] = sw32_add(a[k], a[k + 4]);
#pragma unroll
      for (int k = 0; k < 2; ++k) { u2[k] = sw16_add(t4[k], t4[k + 2]); u2[k] += dpp<XOR8>(u2[k]); }
      const int rho = lane >> 4;
      const int cp = wave * 32 + 4 * c8 + 2 * (rho >> 1) + (rho & 1);
      if ((lane & 8) == 0)
        __builtin_amdgcn_raw_buffer_store_b128(u32x4{__float_as_uint(u2[0]), tag, __float_as_uint(u2[1]), tag}, rs, O_OFF + (unsigned)((cp * 256 + b) * 16), 0, AUX_SC1);
    }
    STAMP(18);
    if (wave == 0) STAMP_OUT(10, 19);
  } else {
    STAMP(0);
    const unsigned epoch = __hip_atomic_load(reinterpret_cast<const unsigned*>(ws + EPOCH_OFF), __ATOMIC_RELAXED, __HIP_MEMORY_SCOPE_AGENT);
    __builtin_amdgcn_s_barrier(); __builtin_amdgcn_s_barrier(); __builtin_amdgcn_s_barrier();
    const unsigned tag = epoch + 1u;
    if (lane == 0) lds_tag = tag;
    STAMP(1);
    __syncthreads();
    STAMP(2);
    if (lane < 4) {
      f32x4 a = *reinterpret_cast<const f32x4*>(&lds_qpart[lane * 4]);
#pragma unroll
      for (int w = 1; w < NW; ++w) a += *reinterpret_cast<const f32x4*>(&lds_qpart[w * 16 + lane * 4]);
      __builtin_amdgcn_raw_buffer_store_b128(u32x4{packbf(a[0], a[1]), tag, packbf(a[2], a[3]), tag}, rs, Q_OFF + (unsigned)(g * 2048 + s * 64 + lane * 16), 0, AUX_SC1);
    }
    STAMP(3);
    {
      u32x4 v0 = {0, 0, 0, 0}, v1 = {0, 0, 0, 0};
      bool ok = false;
      for (unsigned spins = 0; spins < SPIN_CAP; ++spins) {
        CBAR();
        if (!ok) {
          v0 = __builtin_amdgcn_raw_buffer_load_b128(rs, Q_OFF + (unsigned)(g * 2048 + lane * 16), 0, AUX_SC1);
          v1 = __builtin_amdgcn_raw_buffer_load_b128(rs, Q_OFF + (unsigned)(g * 2048 + 1024 + lane * 16), 0, AUX_SC1);
          ok = (v0[1] == tag) & (v0[3] == tag) & (v1[1] == tag) & (v1[3] == tag);
        }
        if (__all(ok)) break;
      }
      *reinterpret_cast<u32x2*>(&lds_q[lane * 2]) = u32x2{v0[0], v0[2]};
      *reinterpret_cast<u32x2*>(&lds_q[128 + lane * 2]) = u32x2{v1[0], v1[2]};
    }
    STAMP(4);
    __syncthreads();
    __syncthreads();
    __syncthreads();
    STAMP(5);
    {
      const int hg = g * 4 + (s >> 3), j = s & 7;
      u32x4 pv[4], ml[4];
      bool ok = false;
      for (unsigned spins = 0; spins < SPIN_CAP; ++spins) {
        CBAR();
        if (!ok) {
#pragma unroll
          for (int i = 0; i < 4; ++i) {
            pv[i] = __builtin_amdgcn_raw_buffer_load_b128(rs, P_OFF + (unsigned)((hg * 8 + j) * 4096 + (lane + 64 * i) * 16), 0, AUX_SC1);
            ml[i] = __builtin_amdgcn_raw_buffer_load_b128(rs, ML_OFF + (unsigned)((hg * 32 + (lane >> 3) + 8 * i) * 16), 0, AUX_SC1);
          }
          ok = true;
#pragma unroll
          for (int i = 0; i < 4; ++i) ok = ok & (pv[i][1] == tag) & (pv[i][3] == tag) & (ml[i][1] == tag) & (ml[i][3] == tag);
        }
        if (__all(ok)) break;
      }
      STAMP(6);
      float M = fmaxf(fmaxf(__uint_as_float(ml[0][0]), __uint_as_float(ml[1][0])), fmaxf(__uint_as_float(ml[2][0]), __uint_as_float(ml[3][0])));
      M = fmaxf(M, dpp<XOR8>(M)); M = sw16_max(M); M = sw32_max(M);
      float L = 0.f, o0 = 0.f, o1 = 0.f;
#pragma unroll
      for (int i = 0; i < 4; ++i) {
        const float wt = __builtin_amdgcn_exp2f(__uint_as_float(ml[i][0]) - M);
        L = fmaf(wt, __uint_as_float(ml[i][2]), L);
        o0 = fmaf(wt, __uint_as_float(pv[i][0]), o0);
        o1 = fmaf(wt, __uint_as_float(pv[i][2]), o1);
      }
      L += dpp<XOR8>(L); L = sw16_add(L, L); L = sw32_add(L, L);
      o0 += dpp<XOR8>(o0); o0 = sw16_add(o0, o0); o0 = sw32_add(o0, o0);
      o1 += dpp<XOR8>(o1); o1 = sw16_add(o1, o1); o1 = sw32_add(o1, o1);
      if (lane < 8) *reinterpret_cast<f32x2*>(&lds_o16[lane * 2]) = f32x2{rbf(o0 / L), rbf(o1 / L)};
    }
    __syncthreads();
    STAMP(7);
    {
      u32x4 ov[4];
      bool ok = false;
      for (unsigned spins = 0; spins < SPIN_CAP; ++spins) {
        CBAR();
        if (!ok) {
#pragma unroll
          for (int i = 0; i < 4; ++i) ov[i] = __builtin_amdgcn_raw_buffer_load_b128(rs, O_OFF + (unsigned)((b * 256 + lane + 64 * i) * 16), 0, AUX_SC1);
          ok = true;
#pragma unroll
          for (int i = 0; i < 4; ++i) ok = ok & (ov[i][1] == tag) & (ov[i][3] == tag);
        }
        if (__all(ok)) break;
      }
      STAMP(8);
      float a0 = (__uint_as_float(ov[0][0]) + __uint_as_float(ov[1][0])) + (__uint_as_float(ov[2][0]) + __uint_as_float(ov[3][0]));
      float a1 = (__uint_as_float(ov[0][2]) + __uint_as_float(ov[1][2])) + (__uint_as_float(ov[2][2]) + __uint_as_float(ov[3][2]));
      a0 += dpp<XOR1>(a0); a0 += dpp<XOR2>(a0); a0 += dpp<ROR4>(a0); a0 += dpp<XOR8>(a0); a0 = sw16_add(a0, a0); a0 = sw32_add(a0, a0);
      a1 += dpp<XOR1>(a1); a1 += dpp<XOR2>(a1); a1 += dpp<ROR4>(a1); a1 += dpp<XOR8>(a1); a1 = sw16_add(a1, a1); a1 = sw32_add(a1, a1);
      if (lane == 0) {
        out[b] = packbf(a0, a1);
        if (b == 0) __hip_atomic_store(reinterpret_cast<unsigned*>(ws + EPOCH_OFF), tag, __ATOMIC_RELAXED, __HIP_MEMORY_SCOPE_AGENT);
      }
    }
    STAMP(9);
    STAMP_OUT(0, 10);
  }
}

extern "C" void kernel_launch(void* const* d_in, const int* in_sizes, int n_in, void* d_out, int out_size, void* d_ws, size_t ws_size, hipStream_t stream) {
  fused_decode<<<256, NT, 0, stream>>>((const u16*)d_in[0], (const u16*)d_in[1], (const u16*)d_in[2], (const u16*)d_in[3], (const u16*)d_in[4],
                                       (unsigned*)d_out, (unsigned char*)d_ws);
}
```

```cpp
#include <hip/hip_runtime.h>
#include <hip/hip_bf16.h>
#include <cstdint>

typedef unsigned u32x4 __attribute__((ext_vector_type(4)));
typedef unsigned u32x2 __attribute__((ext_vector_type(2)));
typedef float f32x4 __attribute__((ext_vector_type(4)));
typedef float f32x2 __attribute__((ext_vector_type(2)));
typedef __bf16 bf16x8 __attribute__((ext_vector_type(8)));
typedef short v4i16_t __attribute__((ext_vector_type(4)));
typedef unsigned short u16;
#define LAS __attribute__((address_space(3)))

constexpr int D_MODEL = 512, HQ = 32, HKV = 8, SK = 32768, DH = 128, W = 4096;
constexpr int NSPLIT = 32, KPS = W / NSPLIT;
constexpr int NW = 8, NT = (NW + 1) * 64;
constexpr unsigned SPIN_CAP = 1u << 22;

constexpr unsigned EPOCH_OFF = 0;
constexpr unsigned Q_OFF = 4096;
constexpr unsigned ML_OFF = Q_OFF + 16384;
constexpr unsigned P_OFF = ML_OFF + 16384;
constexpr unsigned O_OFF = P_OFF + (1u << 20);
constexpr unsigned WS_BYTES = O_OFF + (1u << 18);
constexpr int AUX_SC1 = 16;
#define STAMP(k)
#define STAMP_DECL
#define STAMP_OUT(lo, hi)

__device__ __forceinline__ float bflo(unsigned w) { return __uint_as_float(w << 16); }
__device__ __forceinline__ float bfhi(unsigned w) { return __uint_as_float(w & 0xffff0000u); }
__device__ __forceinline__ float rbf(float x) { return __bfloat162float(__float2bfloat16(x)); }
__device__ __forceinline__ unsigned packbf(float lo, float hi) {
  return (unsigned)__bfloat16_as_ushort(__float2bfloat16(lo)) | ((unsigned)__bfloat16_as_ushort(__float2bfloat16(hi)) << 16);
}
template <int CTRL> __device__ __forceinline__ float dpp(float x) {
  return __builtin_bit_cast(float, __builtin_amdgcn_mov_dpp(__builtin_bit_cast(int, x), CTRL, 0xf, 0xf, true));
}
constexpr int XOR1 = 0xB1, XOR2 = 0x4E, XOR7 = 0x141, XOR8 = 0x128, ROR4 = 0x124;
__device__ __forceinline__ float sw16_add(float a, float b) {
  auto s = __builtin_amdgcn_permlane16_swap(__float_as_uint(a), __float_as_uint(b), false, false);
  return __uint_as_float(s[0]) + __uint_as_float(s[1]);
}
__device__ __forceinline__ float sw32_add(float a, float b) {
  auto s = __builtin_amdgcn_permlane32_swap(__float_as_uint(a), __float_as_uint(b), false, false);
  return __uint_as_float(s[0]) + __uint_as_float(s[1]);
}
__device__ __forceinline__ float sw16_max(float a) {
  auto s = __builtin_amdgcn_permlane16_swap(__float_as_uint(a), __float_as_uint(a), false, false);
  return fmaxf(__uint_as_float(s[0]), __uint_as_float(s[1]));
}
__device__ __forceinline__ float sw32_max(float a) {
  auto s = __builtin_amdgcn_permlane32_swap(__float_as_uint(a), __float_as_uint(a), false, false);
  return fmaxf(__uint_as_float(s[0]), __uint_as_float(s[1]));
}
__device__ __forceinline__ u32x4 ldnt(const u16* p) { return __builtin_nontemporal_load(reinterpret_cast<const u32x4*>(p)); }
__device__ __forceinline__ unsigned off_b(unsigned row, unsigned ch) { return 256u * row + 16u * (ch ^ (((row & 3) << 2) | ((row >> 2) & 3))); }
__device__ __forceinline__ float sel4(f32x4 d, int g) { return g == 0 ? d[0] : g == 1 ? d[1] : g == 2 ? d[2] : d[3]; }
#define CBAR() asm volatile("" ::: "memory")

struct Lds {
  unsigned char v[128 * 256];
  float qpart[NW * 16];
  unsigned q[256];
  u16 pm[4 * 128];
  float m[4 * 8];
  float l[4 * 8];
  float o64[64];
  unsigned tag;
  unsigned pad[3];
};

template <bool ST3>
__device__ __forceinline__ void worker(Lds& L_, const u16* x, const u16* kc, const u16* vc,
                                       const u16* wq, const u16* wo, unsigned char* ws,
                                       const __amdgpu_buffer_rsrc_t rs, const int g, const int s, const int wave, const int lane) {
  constexpr float LOG2E = 1.4426950408889634f;
  constexpr float SCALE = 0.08838834764831845f;
  STAMP_DECL;
  STAMP(10);
  const int idx = g * 512 + s * 16;
  const int G = lane >> 4, n = lane & 15;
  const int qr0 = wave * 64 + (lane >> 1), hf = lane & 1;
  u32x4 wq0 = ldnt(wq + (size_t)qr0 * 4096 + idx + hf * 8);
  u32x4 wq1 = ldnt(wq + (size_t)(qr0 + 32) * 4096 + idx + hf * 8);
  unsigned x0 = x[qr0], x1 = x[qr0 + 32];
  CBAR(); __builtin_amdgcn_s_barrier(); CBAR();
  const size_t kvrow = (size_t)g * SK + (SK - W) + (size_t)s * KPS + wave * 16;
  u32x4 K[4], V[4];
#pragma unroll
  for (int t = 0; t < 4; ++t) K[t] = ldnt(kc + (kvrow + n) * DH + 32 * t + 8 * G);
  asm volatile("" : "+v"(wq0), "+v"(wq1), "+v"(x0), "+v"(x1) :: "memory");
  {
    const float xf0 = bflo(x0), xf1 = bflo(x1);
    float a[8];
#pragma unroll
    for (int k = 0; k < 4; ++k) {
      a[2 * k] = xf0 * bflo(wq0[k]) + xf1 * bflo(wq1[k]);
      a[2 * k + 1] = xf0 * bfhi(wq0[k]) + xf1 * bfhi(wq1[k]);
    }
#pragma unroll
    for (int k = 0; k < 8; ++k) {
      float t = a[k];
      t += dpp<XOR2>(t); t += dpp<ROR4>(t); t += dpp<XOR8>(t);
      t = sw16_add(t, t); t = sw32_add(t, t);
      a[k] = t;
    }
    if (lane < 2) {
      *reinterpret_cast<f32x4*>(&L_.qpart[wave * 16 + lane * 8]) = f32x4{a[0], a[1], a[2], a[3]};
      *reinterpret_cast<f32x4*>(&L_.qpart[wave * 16 + lane * 8 + 4]) = f32x4{a[4], a[5], a[6], a[7]};
    }
  }
  STAMP(11);
  __syncthreads();
  CBAR(); __builtin_amdgcn_s_barrier(); CBAR();
#pragma unroll
  for (int i = 0; i < 4; ++i) V[i] = ldnt(vc + (kvrow + 4 * i + G) * DH + 8 * n);
  CBAR();
  __syncthreads();
  CBAR();
  const int r8 = lane >> 3, c8 = lane & 7;
  const int row0 = g * 512 + (s >> 2) * 64;
  u32x4 wov[8];
  if constexpr (ST3) {
#pragma unroll
    for (int i = 0; i < 8; ++i) wov[i] = ldnt(wo + (size_t)(row0 + r8 + 8 * i) * 512 + wave * 64 + c8 * 8);
  }
  CBAR();
  STAMP(12);
  const unsigned tag = L_.tag;
  f32x4 sacc = {0.f, 0.f, 0.f, 0.f};
#pragma unroll
  for (int t = 0; t < 4; ++t) {
    const u32x4 qa = *reinterpret_cast<const u32x4*>(&L_.q[(lane & 3) * 64 + 16 * t + 4 * G]);
    sacc = __builtin_amdgcn_mfma_f32_16x16x32_bf16(__builtin_bit_cast(bf16x8, qa), __builtin_bit_cast(bf16x8, K[t]), sacc, 0, 0, 0);
  }
  const float sc = rbf(rbf(sel4(sacc, G)) * SCALE) * LOG2E;
  float m = sc; m = fmaxf(m, dpp<XOR1>(m)); m = fmaxf(m, dpp<XOR2>(m)); m = fmaxf(m, dpp<ROR4>(m)); m = fmaxf(m, dpp<XOR8>(m));
  if (n == 0) L_.m[G * 8 + wave] = m;
  STAMP(13);
  __syncthreads();
  {
    const f32x4 m0 = *reinterpret_cast<const f32x4*>(&L_.m[G * 8]), m1 = *reinterpret_cast<const f32x4*>(&L_.m[G * 8 + 4]);
    m = fmaxf(fmaxf(fmaxf(m0[0], m0[1]), fmaxf(m0[2], m0[3])), fmaxf(fmaxf(m1[0], m1[1]), fmaxf(m1[2], m1[3])));
  }
  const float p = __builtin_amdgcn_exp2f(sc - m);
  L_.pm[G * 128 + wave * 16 + n] = __bfloat16_as_ushort(__float2bfloat16(p));
  float l = p; l += dpp<XOR1>(l); l += dpp<XOR2>(l); l += dpp<ROR4>(l); l += dpp<XOR8>(l);
  if (n == 0) L_.l[G * 8 + wave] = l;
#pragma unroll
  for (int i = 0; i < 4; ++i) *reinterpret_cast<u32x4*>(&L_.v[off_b(wave * 16 + 4 * i + G, n)]) = V[i];
  STAMP(14);
  __syncthreads();
  STAMP(15);
  f32x4 oacc = {0.f, 0.f, 0.f, 0.f};
  {
    const unsigned q_ = (lane & 15) >> 2, p_ = lane & 3;
#pragma unroll
    for (int ks = 0; ks < 4; ++ks) {
      const u32x4 pa = *reinterpret_cast<const u32x4*>(&L_.pm[(lane & 3) * 128 + 32 * ks + 8 * G]);
      const v4i16_t b0 = __builtin_amdgcn_ds_read_tr16_b64_v4i16((LAS v4i16_t*)(&L_.v[off_b(32 * ks + 8 * G + q_, 2 * wave + (p_ >> 1)) + 8 * (p_ & 1)]));
      const v4i16_t b1 = __builtin_amdgcn_ds_read_tr16_b64_v4i16((LAS v4i16_t*)(&L_.v[off_b(32 * ks + 8 * G + 4 + q_, 2 * wave + (p_ >> 1)) + 8 * (p_ & 1)]));
      const u32x2 b0u = __builtin_bit_cast(u32x2, b0), b1u = __builtin_bit_cast(u32x2, b1);
      const u32x4 vb = {b0u[0], b0u[1], b1u[0], b1u[1]};
      oacc = __builtin_amdgcn_mfma_f32_16x16x32_bf16(__builtin_bit_cast(bf16x8, pa), __builtin_bit_cast(bf16x8, vb), oacc, 0, 0, 0);
    }
  }
  {
    const float ov = sel4(oacc, G);
    const float on = dpp<XOR1>(ov);
    const int hg = g * 4 + G;
    if ((n & 1) == 0)
      __builtin_amdgcn_raw_buffer_store_b128(u32x4{__float_as_uint(ov), tag, __float_as_uint(on), tag}, rs,
                                             P_OFF + (unsigned)(((hg * 8 + wave) * 32 + s) * 128 + n * 8), 0, AUX_SC1);
    if (wave == 0 && n == 0) {
      const f32x4 l0 = *reinterpret_cast<const f32x4*>(&L_.l[G * 8]), l1 = *reinterpret_cast<const f32x4*>(&L_.l[G * 8 + 4]);
      const float L = ((l0[0] + l0[1]) + (l0[2] + l0[3])) + ((l1[0] + l1[1]) + (l1[2] + l1[3]));
      __builtin_amdgcn_raw_buffer_store_b128(u32x4{__float_as_uint(m), tag, __float_as_uint(L), tag}, rs, ML_OFF + (unsigned)((hg * 32 + s) * 16), 0, AUX_SC1);
    }
  }
  STAMP(16);
  if constexpr (ST3) {
    if (wave < 4) {
      const int u = s >> 2;
      const int hg = g * 4 + (u >> 1), j = (u & 1) * 4 + wave;
      u32x4 pv[4], ml[4];
      bool ok = false;
      for (unsigned spins = 0; spins < SPIN_CAP; ++spins) {
        CBAR();
        if (!ok) {
#pragma unroll
          for (int i = 0; i < 4; ++i) {
            pv[i] = __builtin_amdgcn_raw_buffer_load_b128(rs, P_OFF + (unsigned)((hg * 8 + j) * 4096 + (lane + 64 * i) * 16), 0, AUX_SC1);
            ml[i] = __builtin_amdgcn_raw_buffer_load_b128(rs, ML_OFF + (unsigned)((hg * 32 + (lane >> 3) + 8 * i) * 16), 0, AUX_SC1);
          }
          ok = true;
#pragma unroll
          for (int i = 0; i < 4; ++i) ok = ok & (pv[i][1] == tag) & (pv[i][3] == tag) & (ml[i][1] == tag) & (ml[i][3] == tag);
        }
        if (__all(ok)) break;
      }
      STAMP(19);
      float M = fmaxf(fmaxf(__uint_as_float(ml[0][0]), __uint_as_float(ml[1][0])), fmaxf(__uint_as_float(ml[2][0]), __uint_as_float(ml[3][0])));
      M = fmaxf(M, dpp<XOR8>(M)); M = sw16_max(M); M = sw32_max(M);
      float L = 0.f, o0 = 0.f, o1 = 0.f;
#pragma unroll
      for (int i = 0; i < 4; ++i) {
        const float wt = __builtin_amdgcn_exp2f(__uint_as_float(ml[i][0]) - M);
        L = fmaf(wt, __uint_as_float(ml[i][2]), L);
        o0 = fmaf(wt, __uint_as_float(pv[i][0]), o0);
        o1 = fmaf(wt, __uint_as_float(pv[i][2]), o1);
      }
      L += dpp<XOR8>(L); L = sw16_add(L, L); L = sw32_add(L, L);
      o0 += dpp<XOR8>(o0); o0 = sw16_add(o0, o0); o0 = sw32_add(o0, o0);
      o1 += dpp<XOR8>(o1); o1 = sw16_add(o1, o1); o1 = sw32_add(o1, o1);
      if (lane < 8) *reinterpret_cast<f32x2*>(&L_.o64[wave * 16 + lane * 2]) = f32x2{rbf(o0 / L), rbf(o1 / L)};
    }
    __syncthreads();
    STAMP(17);
    float a[8];
#pragma unroll
    for (int k = 0; k < 8; ++k) a[k] = 0.f;
#pragma unroll
    for (int i = 0; i < 8; ++i) {
      const float ov = L_.o64[r8 + 8 * i];
#pragma unroll
      for (int k = 0; k < 4; ++k) {
        a[2 * k] = fmaf(ov, bflo(wov[i][k]), a[2 * k]);
        a[2 * k + 1] = fmaf(ov, bfhi(wov[i][k]), a[2 * k + 1]);
      }
    }
    float t4[4], u2[2];
#pragma unroll
    for (int k = 0; k < 4; ++k) t4[k] = sw32_add(a[k], a[k + 4]);
#pragma unroll
    for (int k = 0; k < 2; ++k) { u2[k] = sw16_add(t4[k], t4[k + 2]); u2[k] += dpp<XOR8>(u2[k]); }
    const int rho = lane >> 4;
    const int col = wave * 64 + 8 * c8 + 4 * (rho >> 1) + 2 * (rho & 1);
    const int p_ = g * 8 + (s >> 2);
    if ((lane & 8) == 0)
      __builtin_amdgcn_raw_buffer_store_b128(u32x4{__float_as_uint(u2[0]), tag, __float_as_uint(u2[1]), tag}, rs,
                                             O_OFF + (unsigned)((((col >> 4) * 64 + p_) * 16 + (col & 15)) * 8), 0, AUX_SC1);
    STAMP(18);
  }
  if (wave == 0) STAMP_OUT(10, 20);
}

__global__ void __launch_bounds__(NT) fused_decode(const u16* x, const u16* kc, const u16* vc,
                                                   const u16* wq, const u16* wo, unsigned* out,
                                                   unsigned char* ws) {
  __shared__ __attribute__((aligned(16))) Lds L_;
  const int b = blockIdx.x, g = b & 7, s = b >> 3;
  const int tid = threadIdx.x, lane = tid & 63, wave = __builtin_amdgcn_readfirstlane(tid >> 6);
  const __amdgpu_buffer_rsrc_t rs = __builtin_amdgcn_make_buffer_rsrc((void*)ws, 0, (int)WS_BYTES, 0x00020000);

  if (wave < NW) {
    if ((s & 3) == 0) worker<true>(L_, x, kc, vc, wq, wo, ws, rs, g, s, wave, lane);
    else worker<false>(L_, x, kc, vc, wq, wo, ws, rs, g, s, wave, lane);
  } else {
    STAMP_DECL;
    STAMP(0);
    const unsigned epoch = __hip_atomic_load(reinterpret_cast<const unsigned*>(ws + EPOCH_OFF), __ATOMIC_RELAXED, __HIP_MEMORY_SCOPE_AGENT);
    __builtin_amdgcn_s_barrier();
    const unsigned tag = epoch + 1u;
    if (lane == 0) L_.tag = tag;
    STAMP(1);
    __syncthreads();
    STAMP(2);
    if (lane < 4) {
      f32x4 a = *reinterpret_cast<const f32x4*>(&L_.qpart[lane * 4]);
#pragma unroll
      for (int w = 1; w < NW; ++w) a += *reinterpret_cast<const f32x4*>(&L_.qpart[w * 16 + lane * 4]);
      __builtin_amdgcn_raw_buffer_store_b128(u32x4{packbf(a[0], a[1]), tag, packbf(a[2], a[3]), tag}, rs, Q_OFF + (unsigned)(g * 2048 + s * 64 + lane * 16), 0, AUX_SC1);
    }
    CBAR(); __builtin_amdgcn_s_barrier(); CBAR();
    STAMP(3);
    {
      u32x4 v0 = {0, 0, 0, 0}, v1 = {0, 0, 0, 0};
      bool ok = false;
      for (unsigned spins = 0; spins < SPIN_CAP; ++spins) {
        CBAR();
        if (!ok) {
          v0 = __builtin_amdgcn_raw_buffer_load_b128(rs, Q_OFF + (unsigned)(g * 2048 + lane * 16), 0, AUX_SC1);
          v1 = __builtin_amdgcn_raw_buffer_load_b128(rs, Q_OFF + (unsigned)(g * 2048 + 1024 + lane * 16), 0, AUX_SC1);
          ok = (v0[1] == tag) & (v0[3] == tag) & (v1[1] == tag) & (v1[3] == tag);
        }
        if (__all(ok)) break;
      }
      *reinterpret_cast<u32x2*>(&L_.q[lane * 2]) = u32x2{v0[0], v0[2]};
      *reinterpret_cast<u32x2*>(&L_.q[128 + lane * 2]) = u32x2{v1[0], v1[2]};
    }
    STAMP(4);
    __syncthreads();
    __syncthreads();
    __syncthreads();
    STAMP(5);
    if ((s & 3) == 0) __syncthreads();
    STAMP(7);
    if ((s & 3) == 1 && s < 16) {
      const int c = g * 4 + (s >> 2);
      u32x4 ov[8];
      bool ok = false;
      for (unsigned spins = 0; spins < SPIN_CAP; ++spins) {
        CBAR();
        if (!ok) {
#pragma unroll
          for (int i = 0; i < 8; ++i) ov[i] = __builtin_amdgcn_raw_buffer_load_b128(rs, O_OFF + (unsigned)(c * 8192 + (lane + 64 * i) * 16), 0, AUX_SC1);
          ok = true;
#pragma unroll
          for (int i = 0; i < 8; ++i) ok = ok & (ov[i][1] == tag) & (ov[i][3] == tag);
        }
        if (__all(ok)) break;
      }
      STAMP(8);
      float a0 = 0.f, a1 = 0.f;
#pragma unroll
      for (int i = 0; i < 8; ++i) { a0 += __uint_as_float(ov[i][0]); a1 += __uint_as_float(ov[i][2]); }
      a0 += dpp<XOR8>(a0); a0 = sw16_add(a0, a0); a0 = sw32_add(a0, a0);
      a1 += dpp<XOR8>(a1); a1 = sw16_add(a1, a1); a1 = sw32_add(a1, a1);
      if (lane < 8) out[c * 8 + lane] = packbf(a0, a1);
      if (c == 0 && lane == 0) __hip_atomic_store(reinterpret_cast<unsigned*>(ws + EPOCH_OFF), tag, __ATOMIC_RELAXED, __HIP_MEMORY_SCOPE_AGENT);
    }
    STAMP(9);
    STAMP_OUT(0, 10);
  }
}

extern "C" void kernel_launch(void* const* d_in, const int* in_sizes, int n_in, void* d_out, int out_size, void* d_ws, size_t ws_size, hipStream_t stream) {
  fused_decode<<<256, NT, 0, stream>>>((const u16*)d_in[0], (const u16*)d_in[1], (const u16*)d_in[2], (const u16*)d_in[3], (const u16*)d_in[4],
                                       (unsigned*)d_out, (unsigned char*)d_ws);
}
```

```cpp
#include <hip/hip_runtime.h>
#include <hip/hip_bf16.h>
#include <cstdint>

typedef unsigned u32x4 __attribute__((ext_vector_type(4)));
typedef unsigned u32x2 __attribute__((ext_vector_type(2)));
typedef float f32x4 __attribute__((ext_vector_type(4)));
typedef float f32x2 __attribute__((ext_vector_type(2)));
typedef __bf16 bf16x8 __attribute__((ext_vector_type(8)));
typedef short v4i16_t __attribute__((ext_vector_type(4)));
typedef unsigned short u16;
#define LAS __attribute__((address_space(3)))

constexpr int SK = 32768, DH = 128, W = 4096;
constexpr int NSPLIT = 32, KPS = W / NSPLIT;
constexpr int NW = 8, NC = 4, NT = (NW + NC) * 64;
constexpr unsigned SPIN_CAP = 1u << 20;
#define VSPLIT 3
#define D1 8
#define D2 24
#define D3 0
#define SLEEP(n) do { if ((n) > 0) __builtin_amdgcn_s_sleep(n); } while (0)

constexpr unsigned EPOCH_OFF = 0;
constexpr unsigned Q_OFF = 4096;
constexpr unsigned ML_OFF = Q_OFF + 131072;
constexpr unsigned P_OFF = ML_OFF + 16384;
constexpr unsigned O_OFF = P_OFF + (1u << 20);
constexpr unsigned WS_BYTES = O_OFF + (1u << 18);
constexpr int AUX_SC1 = 16;

__device__ __forceinline__ float bflo(unsigned w) { return __uint_as_float(w << 16); }
__device__ __forceinline__ float bfhi(unsigned w) { return __uint_as_float(w & 0xffff0000u); }
__device__ __forceinline__ float rbf(float x) { return __bfloat162float(__float2bfloat16(x)); }
__device__ __forceinline__ unsigned packbf(float lo, float hi) {
  return (unsigned)__bfloat16_as_ushort(__float2bfloat16(lo)) | ((unsigned)__bfloat16_as_ushort(__float2bfloat16(hi)) << 16);
}
template <int CTRL> __device__ __forceinline__ float dpp(float x) {
  return __builtin_bit_cast(float, __builtin_amdgcn_mov_dpp(__builtin_bit_cast(int, x), CTRL, 0xf, 0xf, true));
}
constexpr int XOR1 = 0xB1, XOR2 = 0x4E, XOR8 = 0x128, ROR4 = 0x124;
__device__ __forceinline__ float sw16_add(float a, float b) {
  auto s = __builtin_amdgcn_permlane16_swap(__float_as_uint(a), __float_as_uint(b), false, false);
  return __uint_as_float(s[0]) + __uint_as_float(s[1]);
}
__device__ __forceinline__ float sw32_add(float a, float b) {
  auto s = __builtin_amdgcn_permlane32_swap(__float_as_uint(a), __float_as_uint(b), false, false);
  return __uint_as_float(s[0]) + __uint_as_float(s[1]);
}
__device__ __forceinline__ float sw16_max(float a) {
  auto s = __builtin_amdgcn_permlane16_swap(__float_as_uint(a), __float_as_uint(a), false, false);
  return fmaxf(__uint_as_float(s[0]), __uint_as_float(s[1]));
}
__device__ __forceinline__ float sw32_max(float a) {
  auto s = __builtin_amdgcn_permlane32_swap(__float_as_uint(a), __float_as_uint(a), false, false);
  return fmaxf(__uint_as_float(s[0]), __uint_as_float(s[1]));
}
__device__ __forceinline__ u32x4 ldnt(const u16* p) { return __builtin_nontemporal_load(reinterpret_cast<const u32x4*>(p)); }
__device__ __forceinline__ unsigned off_b(unsigned row, unsigned ch) { return 256u * row + 16u * (ch ^ (((row & 3) << 2) | ((row >> 2) & 3))); }
__device__ __forceinline__ bf16x8 tr_frag(unsigned char* img, int ks, int c, int lane) {
  const unsigned G = lane >> 4, q_ = (lane & 15) >> 2, p_ = lane & 3;
  const v4i16_t b0 = __builtin_amdgcn_ds_read_tr16_b64_v4i16((LAS v4i16_t*)(&img[off_b(32 * ks + 8 * G + q_, 2 * c + (p_ >> 1)) + 8 * (p_ & 1)]));
  const v4i16_t b1 = __builtin_amdgcn_ds_read_tr16_b64_v4i16((LAS v4i16_t*)(&img[off_b(32 * ks + 8 * G + 4 + q_, 2 * c + (p_ >> 1)) + 8 * (p_ & 1)]));
  const u32x2 b0u = __builtin_bit_cast(u32x2, b0), b1u = __builtin_bit_cast(u32x2, b1);
  return __builtin_bit_cast(bf16x8, u32x4{b0u[0], b0u[1], b1u[0], b1u[1]});
}
#define CBAR() asm volatile("" ::: "memory")

struct Lds {
  unsigned char v[128 * 256];
  float qpart[NW * 64];
  unsigned q[256];
  u16 pm[4 * 128];
  float m[4 * 8];
  float l[4 * 8];
  unsigned o64[32];
  unsigned tag;
  unsigned pad[3];
};

__global__ void __launch_bounds__(NT) fused_decode(const u16* x, const u16* kc, const u16* vc, const u16* wq, const u16* wo, unsigned* out, unsigned char* ws) {
  __shared__ __attribute__((aligned(16))) Lds L_;
  const int b = blockIdx.x, g = b & 7, s = b >> 3;
  const int tid = threadIdx.x, lane = tid & 63, wave = __builtin_amdgcn_readfirstlane(tid >> 6);
  const __amdgpu_buffer_rsrc_t rs = __builtin_amdgcn_make_buffer_rsrc((void*)ws, 0, (int)WS_BYTES, 0x00020000);
  constexpr float LOG2E = 1.4426950408889634f;
  constexpr float SCALE = 0.08838834764831845f;
  const int u = s >> 2, cq = s & 3;

  if (wave < NW) {
    const int G = lane >> 4, n = lane & 15;
    const int qr0 = cq * 128 + wave * 16 + (lane >> 3);
    const u16* wqp = wq + (size_t)qr0 * 4096 + g * 512 + u * 64 + (lane & 7) * 8;
    u32x4 wq0 = ldnt(wqp);
    u32x4 wq1 = ldnt(wqp + 8 * 4096);
    unsigned x0 = x[qr0], x1 = x[qr0 + 8];
    CBAR(); __builtin_amdgcn_s_barrier(); CBAR();
    const size_t kvrow = (size_t)g * SK + (SK - W) + (size_t)s * KPS + wave * 16;
    u32x4 K[4], V[4];
#pragma unroll
    for (int t = 0; t < 4; ++t) K[t] = ldnt(kc + (kvrow + n) * DH + 32 * t + 8 * G);
    asm volatile("" : "+v"(wq0), "+v"(wq1), "+v"(x0), "+v"(x1) :: "memory");
    {
      const float xf0 = bflo(x0), xf1 = bflo(x1);
      float a[8];
#pragma unroll
      for (int k = 0; k < 4; ++k) {
        a[2 * k] = xf0 * bflo(wq0[k]) + xf1 * bflo(wq1[k]);
        a[2 * k + 1] = xf0 * bfhi(wq0[k]) + xf1 * bfhi(wq1[k]);
      }
      float t4[4], u2[2];
#pragma unroll
      for (int k = 0; k < 4; ++k) t4[k] = sw32_add(a[k], a[k + 4]);
#pragma unroll
      for (int k = 0; k < 2; ++k) { u2[k] = sw16_add(t4[k], t4[k + 2]); u2[k] += dpp<XOR8>(u2[k]); }
      if ((lane & 8) == 0) *reinterpret_cast<f32x2*>(&L_.qpart[wave * 64 + 8 * (lane & 7) + 4 * (G >> 1) + 2 * (G & 1)]) = f32x2{u2[0], u2[1]};
    }
    __syncthreads();
    CBAR(); __builtin_amdgcn_s_barrier(); CBAR();
#pragma unroll
    for (int i = 0; i < VSPLIT; ++i) V[i] = ldnt(vc + (kvrow + 4 * i + G) * DH + 8 * n);
    CBAR();
    __syncthreads();
    CBAR();
#pragma unroll
    for (int i = VSPLIT; i < 4; ++i) V[i] = ldnt(vc + (kvrow + 4 * i + G) * DH + 8 * n);
    const u16* wop = wo + (size_t)(g * 512 + u * 64 + wave * 8 + G) * 512 + cq * 128 + n * 8;
    const u32x4 wo0 = ldnt(wop), wo1 = ldnt(wop + 4 * 512);
    CBAR();
    const unsigned tag = L_.tag;
    f32x4 sa = {0.f, 0.f, 0.f, 0.f}, sb = {0.f, 0.f, 0.f, 0.f};
#pragma unroll
    for (int t = 0; t < 4; t += 2) {
      const u32x4 qa = *reinterpret_cast<const u32x4*>(&L_.q[(n >> 2) * 64 + 16 * t + 4 * G]);
      const u32x4 qb = *reinterpret_cast<const u32x4*>(&L_.q[(n >> 2) * 64 + 16 * (t + 1) + 4 * G]);
      sa = __builtin_amdgcn_mfma_f32_16x16x32_bf16(__builtin_bit_cast(bf16x8, qa), __builtin_bit_cast(bf16x8, K[t]), sa, 0, 0, 0);
      sb = __builtin_amdgcn_mfma_f32_16x16x32_bf16(__builtin_bit_cast(bf16x8, qb), __builtin_bit_cast(bf16x8, K[t + 1]), sb, 0, 0, 0);
    }
    const float sc = rbf(rbf(sa[0] + sb[0]) * SCALE) * LOG2E;
    float m = sc; m = fmaxf(m, dpp<XOR1>(m)); m = fmaxf(m, dpp<XOR2>(m)); m = fmaxf(m, dpp<ROR4>(m)); m = fmaxf(m, dpp<XOR8>(m));
    if (n == 0) L_.m[G * 8 + wave] = m;
    __syncthreads();
    {
      const f32x4 m0 = *reinterpret_cast<const f32x4*>(&L_.m[G * 8]), m1 = *reinterpret_cast<const f32x4*>(&L_.m[G * 8 + 4]);
      m = fmaxf(fmaxf(fmaxf(m0[0], m0[1]), fmaxf(m0[2], m0[3])), fmaxf(fmaxf(m1[0], m1[1]), fmaxf(m1[2], m1[3])));
    }
    const float p = __builtin_amdgcn_exp2f(sc - m);
    L_.pm[G * 128 + wave * 16 + n] = __bfloat16_as_ushort(__float2bfloat16(p));
    float l = p; l += dpp<XOR1>(l); l += dpp<XOR2>(l); l += dpp<ROR4>(l); l += dpp<XOR8>(l);
    if (n == 0) L_.l[G * 8 + wave] = l;
#pragma unroll
    for (int i = 0; i < 4; ++i) *reinterpret_cast<u32x4*>(&L_.v[off_b(wave * 16 + 4 * i + G, n)]) = V[i];
    __syncthreads();
    f32x4 oa_ = {0.f, 0.f, 0.f, 0.f}, ob_ = {0.f, 0.f, 0.f, 0.f};
#pragma unroll
    for (int ks = 0; ks < 4; ks += 2) {
      const u32x4 pa = *reinterpret_cast<const u32x4*>(&L_.pm[(n >> 2) * 128 + 32 * ks + 8 * G]);
      const u32x4 pb = *reinterpret_cast<const u32x4*>(&L_.pm[(n >> 2) * 128 + 32 * (ks + 1) + 8 * G]);
      oa_ = __builtin_amdgcn_mfma_f32_16x16x32_bf16(__builtin_bit_cast(bf16x8, pa), tr_frag(L_.v, ks, wave, lane), oa_, 0, 0, 0);
      ob_ = __builtin_amdgcn_mfma_f32_16x16x32_bf16(__builtin_bit_cast(bf16x8, pb), tr_frag(L_.v, ks + 1, wave, lane), ob_, 0, 0, 0);
    }
    {
      const float ov = oa_[0] + ob_[0];
      const float on = dpp<XOR1>(ov);
      const int hg = g * 4 + G;
      if ((n & 1) == 0)
        __builtin_amdgcn_raw_buffer_store_b128(u32x4{__float_as_uint(ov), tag, __float_as_uint(on), tag}, rs,
                                               P_OFF + (unsigned)(((hg * 8 + wave) * 32 + s) * 128 + n * 8), 0, AUX_SC1);
      if (wave == 0 && n == 0) {
        const f32x4 l0 = *reinterpret_cast<const f32x4*>(&L_.l[G * 8]), l1 = *reinterpret_cast<const f32x4*>(&L_.l[G * 8 + 4]);
        const float L = ((l0[0] + l0[1]) + (l0[2] + l0[3])) + ((l1[0] + l1[1]) + (l1[2] + l1[3]));
        __builtin_amdgcn_raw_buffer_store_b128(u32x4{__float_as_uint(m), tag, __float_as_uint(L), tag}, rs, ML_OFF + (unsigned)((hg * 32 + s) * 16), 0, AUX_SC1);
      }
    }
    __syncthreads();
    *reinterpret_cast<u32x4*>(&L_.v[off_b(wave * 8 + G, n)]) = wo0;
    *reinterpret_cast<u32x4*>(&L_.v[off_b(wave * 8 + 4 + G, n)]) = wo1;
    __syncthreads();
    {
      f32x4 acc = {0.f, 0.f, 0.f, 0.f};
#pragma unroll
      for (int ks = 0; ks < 2; ++ks) {
        const u32x4 oa = *reinterpret_cast<const u32x4*>(&L_.o64[16 * ks + 4 * G]);
        acc = __builtin_amdgcn_mfma_f32_16x16x32_bf16(__builtin_bit_cast(bf16x8, oa), tr_frag(L_.v, ks, wave, lane), acc, 0, 0, 0);
      }
      const float v0 = acc[0], v1 = dpp<XOR1>(v0);
      if (lane < 16 && (n & 1) == 0)
        __builtin_amdgcn_raw_buffer_store_b128(u32x4{__float_as_uint(v0), tag, __float_as_uint(v1), tag}, rs,
                                               O_OFF + (unsigned)((((cq * 8 + wave) * 64 + g * 8 + u) * 16 + n) * 8), 0, AUX_SC1);
    }
  } else {
    const int cw = wave - NW;
    const unsigned epoch = __hip_atomic_load(reinterpret_cast<const unsigned*>(ws + EPOCH_OFF), __ATOMIC_RELAXED, __HIP_MEMORY_SCOPE_AGENT);
    __builtin_amdgcn_s_barrier();
    const unsigned tag = epoch + 1u;
    if (cw == 0 && lane == 0) L_.tag = tag;
    __syncthreads();
    if (cw == 0 && lane < 32) {
      f32x2 a = *reinterpret_cast<const f32x2*>(&L_.qpart[lane * 2]);
#pragma unroll
      for (int w = 1; w < NW; ++w) a += *reinterpret_cast<const f32x2*>(&L_.qpart[w * 64 + lane * 2]);
      __builtin_amdgcn_raw_buffer_store_b128(u32x4{__float_as_uint(a[0]), tag, __float_as_uint(a[1]), tag}, rs,
                                             Q_OFF + (unsigned)(g * 16384 + cq * 4096 + u * 512 + lane * 16), 0, AUX_SC1);
    }
    CBAR(); __builtin_amdgcn_s_barrier(); CBAR();
    {
      u32x4 v[4];
      bool ok = false;
      SLEEP(D1);
      for (unsigned spins = 0; spins < SPIN_CAP; ++spins) {
        CBAR();
        if (!ok) {
#pragma unroll
          for (int i = 0; i < 4; ++i) v[i] = __builtin_amdgcn_raw_buffer_load_b128(rs, Q_OFF + (unsigned)(g * 16384 + i * 4096 + cw * 1024 + lane * 16), 0, AUX_SC1);
          ok = true;
#pragma unroll
          for (int i = 0; i < 4; ++i) ok = ok & (v[i][1] == tag) & (v[i][3] == tag);
        }
        if (__all(ok)) break;
      }
      const float q0 = (__uint_as_float(v[0][0]) + __uint_as_float(v[1][0])) + (__uint_as_float(v[2][0]) + __uint_as_float(v[3][0]));
      const float q1 = (__uint_as_float(v[0][2]) + __uint_as_float(v[1][2])) + (__uint_as_float(v[2][2]) + __uint_as_float(v[3][2]));
      L_.q[cw * 64 + lane] = packbf(q0, q1);
    }
    __syncthreads();
    __syncthreads();
    __syncthreads();
    __syncthreads();
    {
      const int hg = g * 4 + (u >> 1), j = (u & 1) * 4 + cw;
      u32x4 pv[4], ml[4];
      bool ok = false;
      SLEEP(D2);
      for (unsigned spins = 0; spins < SPIN_CAP; ++spins) {
        CBAR();
        if (!ok) {
#pragma unroll
          for (int i = 0; i < 4; ++i) {
            pv[i] = __builtin_amdgcn_raw_buffer_load_b128(rs, P_OFF + (unsigned)((hg * 8 + j) * 4096 + (lane + 64 * i) * 16), 0, AUX_SC1);
            ml[i] = __builtin_amdgcn_raw_buffer_load_b128(rs, ML_OFF + (unsigned)((hg * 32 + (lane >> 3) + 8 * i) * 16), 0, AUX_SC1);
          }
          ok = true;
#pragma unroll
          for (int i = 0; i < 4; ++i) ok = ok & (pv[i][1] == tag) & (pv[i][3] == tag) & (ml[i][1] == tag) & (ml[i][3] == tag);
        }
        if (__all(ok)) break;
      }
      float M = fmaxf(fmaxf(__uint_as_float(ml[0][0]), __uint_as_float(ml[1][0])), fmaxf(__uint_as_float(ml[2][0]), __uint_as_float(ml[3][0])));
      M = fmaxf(M, dpp<XOR8>(M)); M = sw16_max(M); M = sw32_max(M);
      float L = 0.f, o0 = 0.f, o1 = 0.f;
#pragma unroll
      for (int i = 0; i < 4; ++i) {
        const float wt = __builtin_amdgcn_exp2f(__uint_as_float(ml[i][0]) - M);
        L = fmaf(wt, __uint_as_float(ml[i][2]), L);
        o0 = fmaf(wt, __uint_as_float(pv[i][0]), o0);
        o1 = fmaf(wt, __uint_as_float(pv[i][2]), o1);
      }
      L += dpp<XOR8>(L); L = sw16_add(L, L); L = sw32_add(L, L);
      o0 += dpp<XOR8>(o0); o0 = sw16_add(o0, o0); o0 = sw32_add(o0, o0);
      o1 += dpp<XOR8>(o1); o1 = sw16_add(o1, o1); o1 = sw32_add(o1, o1);
      if (lane < 8) L_.o64[cw * 8 + lane] = packbf(o0 / L, o1 / L);
    }
    __syncthreads();
    if (cw == 0 && cq == 1 && s < 16) {
      const int c = g * 4 + u;
      u32x4 ov[8];
      bool ok = false;
      SLEEP(D3);
      for (unsigned spins = 0; spins < SPIN_CAP; ++spins) {
        CBAR();
        if (!ok) {
#pragma unroll
          for (int i = 0; i < 8; ++i) ov[i] = __builtin_amdgcn_raw_buffer_load_b128(rs, O_OFF + (unsigned)(c * 8192 + (lane + 64 * i) * 16), 0, AUX_SC1);
          ok = true;
#pragma unroll
          for (int i = 0; i < 8; ++i) ok = ok & (ov[i][1] == tag) & (ov[i][3] == tag);
        }
        if (__all(ok)) break;
      }
      float a0 = 0.f, a1 = 0.f;
#pragma unroll
      for (int i = 0; i < 8; ++i) { a0 += __uint_as_float(ov[i][0]); a1 += __uint_as_float(ov[i][2]); }
      a0 += dpp<XOR8>(a0); a0 = sw16_add(a0, a0); a0 = sw32_add(a0, a0);
      a1 += dpp<XOR8>(a1); a1 = sw16_add(a1, a1); a1 = sw32_add(a1, a1);
      if (lane < 8) out[c * 8 + lane] = packbf(a0, a1);
      if (c == 0 && lane == 0) __hip_atomic_store(reinterpret_cast<unsigned*>(ws + EPOCH_OFF), tag, __ATOMIC_RELAXED, __HIP_MEMORY_SCOPE_AGENT);
    }
  }
}

extern "C" void kernel_launch(void* const* d_in, const int* in_sizes, int n_in, void* d_out, int out_size, void* d_ws, size_t ws_size, hipStream_t stream) {
  fused_decode<<<256, NT, 0, stream>>>((const u16*)d_in[0], (const u16*)d_in[1], (const u16*)d_in[2], (const u16*)d_in[3], (const u16*)d_in[4],
                                       (unsigned*)d_out, (unsigned char*)d_ws);
}
```
